```python
import torch

def _softmax(x, axis=-1):
    return torch.softmax(x, dim=axis)
def _gelu(x):
    return torch.nn.functional.gelu(x, approximate="tanh")
def _silu(x):
    return torch.nn.functional.silu(x)
def _sigmoid(x):
    return torch.sigmoid(x)
def _layernorm(x, axis=-1, eps=1e-5):
    m = x.mean(dim=axis, keepdim=True)
    v = x.var(dim=axis, keepdim=True, unbiased=False)
    return (x - m) / torch.sqrt(v + eps)
def _scatter_add(tgt, idx, src, axis=0):
    return tgt.clone().index_add_(axis, idx, src)
def _segment_sum(src, seg, n):
    out = torch.zeros((n,) + src.shape[1:], dtype=src.dtype)
    return out.index_add_(0, seg, src)
def _pad(x, pw):
    flat = tuple(v for pair in reversed(pw) for v in pair)
    return torch.nn.functional.pad(x, flat)

kernel_name = 'concat_heads__sliding_block__softmax_full_mha__prompt'

def setup_inputs():

    _w = (lambda a: (lambda t: t.to(torch.bfloat16) if t.is_floating_point() else t)(torch.as_tensor(a)))
    def _A_setup():
        B, S, D = 1, 2048, 512
        return {
            "x": _w((lambda *s: torch.randn(*s, dtype=torch.bfloat16) * 0.4)(B, S, D)),
            "wqkv": _w((lambda *s: torch.randn(*s, dtype=torch.bfloat16) * 0.4)(D, 3 * D)),
            "wo": _w((lambda *s: torch.randn(*s, dtype=torch.bfloat16) * 0.05)(D, D)),
        }
    _A_in = _A_setup()

    def _B_setup():
        B, S, D = 1, 2048, 512
        return {
            "x": _w((lambda *s: torch.randn(*s, dtype=torch.bfloat16) * 0.2)(B, S, D)),
            "wqkv": _w((lambda *s: torch.randn(*s, dtype=torch.bfloat16) * 0.4)(D, 3 * D)),
            "wo": _w((lambda *s: torch.randn(*s, dtype=torch.bfloat16) * 0.1)(D, D)),
        }
    _B_in = _B_setup()

    return {
        "A_x": _A_in["x"],
        "A_wqkv": _A_in["wqkv"],
        "A_wo": _A_in["wo"],
        "B_x": _B_in["x"],
        "B_wqkv": _B_in["wqkv"],
        "B_wo": _B_in["wo"],
    }

def reference(A_x, A_wqkv, A_wo, B_x, B_wqkv, B_wo):
    def _A_ref(x, wqkv, wo):
        B, S, D = x.shape
        H = 32; Dh = D // H; W = 256
        qkv = (lambda x, *d: x.permute(*d))((x @ wqkv).reshape(B, S, 3, H, Dh), 2, 0, 3, 1, 4)
        q, k, v = qkv[0], qkv[1], qkv[2]
        scores = torch.einsum("bhqd,bhkd->bhqk", q, k) * (Dh ** -0.5)
        idx = torch.arange(S)
        mask = (idx[:, None] - idx[None, :] >= 0) & (idx[:, None] - idx[None, :] < W)
        scores = torch.where(mask[None, None], scores, -1e9)
        p = _softmax(scores, axis=-1)
        o = torch.einsum("bhqk,bhkd->bhqd", p, v)
        return (lambda x, *d: x.permute(*d))(o, 0, 2, 1, 3).reshape(B, S, D) @ wo

    def _B_ref(x, wqkv, wo):
        B, S, D = x.shape
        H = 32; Dh = D // H
        qkv = (lambda x, *d: x.permute(*d))((x @ wqkv).reshape(B, S, 3, H, Dh), 2, 0, 3, 1, 4)
        q, k, v = qkv[0], qkv[1], qkv[2]
        scale = Dh ** -0.5
        scores = torch.einsum("bhqd,bhkd->bhqk", q, k) * scale
        p = _softmax(scores, axis=-1)
        o = torch.einsum("bhqk,bhkd->bhqd", p, v)
        return (lambda x, *d: x.permute(*d))(o, 0, 2, 1, 3).reshape(B, S, D) @ wo

    a = _A_ref(A_x, A_wqkv, A_wo)
    b = _B_ref(B_x, B_wqkv, B_wo)
    return torch.concatenate([a, b], axis=-1)
```

```cpp
#include <hip/hip_runtime.h>
#include <hip/hip_bf16.h>

typedef unsigned short u16;
using bf16x8 = __attribute__((ext_vector_type(8))) short;
using s16x4 = __attribute__((ext_vector_type(4))) short;
using f32x16 = __attribute__((ext_vector_type(16))) float;
using u32x4 = __attribute__((ext_vector_type(4))) unsigned;
using u32x2 = __attribute__((ext_vector_type(2))) unsigned;
#define DI __device__ __forceinline__
#define MFMA(a, b, c) __builtin_amdgcn_mfma_f32_32x32x16_bf16((a), (b), (c), 0, 0, 0)
typedef __attribute__((address_space(3))) char* lds_ptr;

constexpr int SEQ = 2048, DM = 512, NH = 32, DH = 16;
constexpr float QSCALE = 0.25f * 1.4426950408889634f;
constexpr float THR = 12.0f;

DI unsigned cvtpk(float lo, float hi) {
  typedef __bf16 b2 __attribute__((ext_vector_type(2))); typedef float f2 __attribute__((ext_vector_type(2)));
  f2 v = {lo, hi}; return __builtin_bit_cast(unsigned, __builtin_convertvector(v, b2)); }
DI void glds16(const void* g, void* lds) { __builtin_amdgcn_global_load_lds((const unsigned*)g, (unsigned*)lds, 16, 0, 0); }
DI s16x4 tr_read(const char* p) {
  typedef short v4i16_t __attribute__((ext_vector_type(4)));
  return __builtin_bit_cast(s16x4, __builtin_amdgcn_ds_read_tr16_b64_v4i16((__attribute__((address_space(3))) v4i16_t*)p));
}

template <int BM, int BN, int MB, int NB, bool ISV>
DI void gemm_loop(const u16* __restrict__ Xg, const u16* __restrict__ Wg, int ldw, char* smem, f32x16 (&acc)[MB][NB],
                  int wm, int wn, int tid) {
  constexpr int XB = BM * 128, WB = 64 * BN * 2, STG = XB + WB;
  const int lane = tid & 63, wid = tid >> 6;
  const int r = lane & 31, h = lane >> 5, g = (lane >> 4) & 1, q = (lane & 15) >> 2, p = lane & 3;
  auto stage = [&](int buf, int kt) {
    char* sx = smem + buf * STG;
    char* sw = sx + XB;
#pragma unroll
    for (int i = 0; i < BM / 32; ++i) {
      int row = (i * 4 + wid) * 8 + (lane >> 3);
      int c = (lane & 7) ^ (row & 7);
      glds16(Xg + (size_t)row * DM + kt * 64 + c * 8, sx + (i * 4 + wid) * 1024);
    }
#pragma unroll
    for (int i = 0; i < WB / 4096; ++i) {
      int off = (i * 4 + wid) * 1024 + lane * 16;
      int k = off / (BN * 2), n = (off % (BN * 2)) >> 1;
      glds16(Wg + (size_t)(kt * 64 + k) * ldw + n, sw + (i * 4 + wid) * 1024);
    }
  };
  stage(0, 0); stage(1, 1); stage(2, 2);
#pragma unroll 1
  for (int kt = 0; kt < 8; ++kt) {
    static_assert(BM / 32 + WB / 4096 == 6, "vmcnt counts assume 6 loads/wave/stage");
    if (kt < 6) asm volatile("s_waitcnt vmcnt(12)" ::: "memory");
    else if (kt == 6) asm volatile("s_waitcnt vmcnt(6)" ::: "memory");
    else asm volatile("s_waitcnt vmcnt(0)" ::: "memory");
    asm volatile("s_waitcnt lgkmcnt(0)" ::: "memory");
    __builtin_amdgcn_s_barrier();
    if (kt + 3 < 8) stage((kt + 3) & 3, kt + 3);
    const char* sx = smem + (kt & 3) * STG;
    const char* sw = sx + XB;
#pragma unroll
    for (int ks = 0; ks < 4; ++ks) {
      bf16x8 xf[MB], wf[NB];
#pragma unroll
      for (int m = 0; m < MB; ++m) {
        int row = wm * (MB * 32) + m * 32 + r;
        xf[m] = *(const bf16x8*)(sx + row * 128 + (((ks * 2 + h) ^ (row & 7)) << 4));
      }
#pragma unroll
      for (int n = 0; n < NB; ++n) {
        const char* a = sw + (ks * 16 + 8 * h + q) * (BN * 2) + (wn * (NB * 32) + n * 32 + g * 16 + 4 * p) * 2;
        s16x4 lo = tr_read(a), hi = tr_read(a + 4 * BN * 2);
        wf[n] = __builtin_shufflevector(lo, hi, 0, 1, 2, 3, 4, 5, 6, 7);
      }
#pragma unroll
      for (int m = 0; m < MB; ++m)
#pragma unroll
        for (int n = 0; n < NB; ++n) {
          if (ISV) acc[m][n] = MFMA(xf[m], wf[n], acc[m][n]);
          else     acc[m][n] = MFMA(wf[n], xf[m], acc[m][n]);
        }
    }
  }
}

__global__ __launch_bounds__(384) void qkv_kernel(const u16* __restrict__ xA, const u16* __restrict__ wA,
                                                  const u16* __restrict__ xB, const u16* __restrict__ wB,
                                                  u16* __restrict__ Qo, u16* __restrict__ Ko, u16* __restrict__ Vo) {
  extern __shared__ __attribute__((aligned(16))) char smem[];
  constexpr int BM = 128, BN = 192, MB = 2, NB = 2;
  constexpr int XB = BM * 128, WB = 64 * BN * 2, STG = XB + WB;
  const int bid = blockIdx.x, xcd = bid & 7, jj = bid >> 3, br = xcd >> 2, tm = (xcd & 3) * 4 + (jj >> 3), tn = jj & 7;
  const u16* Xg = (br ? xB : xA) + (size_t)tm * BM * DM;
  const u16* Wg = (br ? wB : wA) + tn * BN;
  const int tid = threadIdx.x, lane = tid & 63, wid = __builtin_amdgcn_readfirstlane(tid >> 6);
  const int wm = wid & 1, wn = wid >> 1;
  const int r = lane & 31, h = lane >> 5, g = (lane >> 4) & 1, q = (lane & 15) >> 2, p = lane & 3;
  const int ncol0 = tn * BN + wn * 64;
  const bool isv = ncol0 >= 1024;
  f32x16 acc[MB][NB];
#pragma unroll
  for (int m = 0; m < MB; ++m)
#pragma unroll
    for (int n = 0; n < NB; ++n) acc[m][n] = f32x16{};
  auto stage = [&](int buf, int kt) {
    char* sx = smem + buf * STG;
    char* sw = sx + XB;
    for (int i = wid; i < BM / 8; i += 6) {
      int row = i * 8 + (lane >> 3);
      int c = (lane & 7) ^ (row & 7);
      glds16(Xg + (size_t)row * DM + kt * 64 + c * 8, sx + i * 1024);
    }
    for (int i = wid; i < WB / 1024; i += 6) {
      int off = i * 1024 + lane * 16;
      int k = off / (BN * 2), n = (off % (BN * 2)) >> 1;
      glds16(Wg + (size_t)(kt * 64 + k) * 1536 + n, sw + i * 1024);
    }
  };
  stage(0, 0); stage(1, 1); stage(2, 2);
#pragma unroll 1
  for (int kt = 0; kt < 8; ++kt) {
    if (kt < 6) { if (wid < 4) asm volatile("s_waitcnt vmcnt(14)" ::: "memory"); else asm volatile("s_waitcnt vmcnt(12)" ::: "memory"); }
    else if (kt == 6) { if (wid < 4) asm volatile("s_waitcnt vmcnt(7)" ::: "memory"); else asm volatile("s_waitcnt vmcnt(6)" ::: "memory"); }
    else asm volatile("s_waitcnt vmcnt(0)" ::: "memory");
    asm volatile("s_waitcnt lgkmcnt(0)" ::: "memory");
    __builtin_amdgcn_s_barrier();
    if (kt + 3 < 8) stage((kt + 3) & 3, kt + 3);
    const char* sx = smem + (kt & 3) * STG;
    const char* sw = sx + XB;
#pragma unroll
    for (int ks = 0; ks < 4; ++ks) {
      bf16x8 xf[MB], wf[NB];
#pragma unroll
      for (int m = 0; m < MB; ++m) {
        int row = wm * 64 + m * 32 + r;
        xf[m] = *(const bf16x8*)(sx + row * 128 + (((ks * 2 + h) ^ (row & 7)) << 4));
      }
#pragma unroll
      for (int n = 0; n < NB; ++n) {
        const char* a = sw + (ks * 16 + 8 * h + q) * (BN * 2) + (wn * 64 + n * 32 + g * 16 + 4 * p) * 2;
        s16x4 lo = tr_read(a), hi = tr_read(a + 4 * BN * 2);
        wf[n] = __builtin_shufflevector(lo, hi, 0, 1, 2, 3, 4, 5, 6, 7);
      }
      if (isv) {
#pragma unroll
        for (int m = 0; m < MB; ++m)
#pragma unroll
          for (int n = 0; n < NB; ++n) acc[m][n] = MFMA(xf[m], wf[n], acc[m][n]);
      } else {
#pragma unroll
        for (int m = 0; m < MB; ++m)
#pragma unroll
          for (int n = 0; n < NB; ++n) acc[m][n] = MFMA(wf[n], xf[m], acc[m][n]);
      }
    }
  }
  const int row0 = tm * BM + wm * 64;
  if (isv) {
#pragma unroll
    for (int m = 0; m < MB; ++m)
#pragma unroll
      for (int n = 0; n < NB; ++n) {
        int col = ncol0 - 1024 + n * 32 + r, head = col >> 4, d = col & 15;
        int tile = (row0 + m * 32) >> 5;
        u16* dst = Vo + ((size_t)(br * NH + head) * 64 + tile) * 512 + d * 16 + h * 8;
#pragma unroll
        for (int s = 0; s < 2; ++s) {
          u32x4 v = {cvtpk(acc[m][n][8 * s + 0], acc[m][n][8 * s + 1]), cvtpk(acc[m][n][8 * s + 2], acc[m][n][8 * s + 3]),
                     cvtpk(acc[m][n][8 * s + 4], acc[m][n][8 * s + 5]), cvtpk(acc[m][n][8 * s + 6], acc[m][n][8 * s + 7])};
          *(u32x4*)(dst + s * 256) = v;
        }
      }
  } else {
    const bool isq = ncol0 < 512;
    const float sc = isq ? QSCALE : 1.0f;
    u16* base = isq ? Qo : Ko;
#pragma unroll
    for (int m = 0; m < MB; ++m)
#pragma unroll
      for (int n = 0; n < NB; ++n) {
        int head0 = ((ncol0 & 511) + n * 32) >> 4;
        int tok = row0 + m * 32 + r;
#pragma unroll
        for (int s = 0; s < 2; ++s) {
          u32x4 v = {cvtpk(acc[m][n][8 * s + 0] * sc, acc[m][n][8 * s + 1] * sc), cvtpk(acc[m][n][8 * s + 2] * sc, acc[m][n][8 * s + 3] * sc),
                     cvtpk(acc[m][n][8 * s + 4] * sc, acc[m][n][8 * s + 5] * sc), cvtpk(acc[m][n][8 * s + 6] * sc, acc[m][n][8 * s + 7] * sc)};
          *(u32x4*)(base + ((size_t)(br * NH + head0 + s) * SEQ + tok) * 16 + h * 8) = v;
        }
      }
  }
}

__global__ __launch_bounds__(256) void oproj_kernel(const u16* __restrict__ O, const u16* __restrict__ woA, const u16* __restrict__ woB,
                                                    u16* __restrict__ out) {
  extern __shared__ __attribute__((aligned(16))) char smem[];
  constexpr int BM = 64, BN = 128, MB = 1, NB = 2;
  const int bid = blockIdx.x, xcd = bid & 7, jj = bid >> 3, br = xcd >> 2, tm = (xcd & 3) * 8 + (jj >> 2), tn = jj & 3;
  const u16* Xg = O + ((size_t)br * SEQ + tm * BM) * DM;
  const u16* Wg = (br ? woB : woA) + tn * BN;
  const int tid = threadIdx.x, lane = tid & 63, wid = __builtin_amdgcn_readfirstlane(tid >> 6);
  const int wm = wid & 1, wn = wid >> 1, r = lane & 31, h = lane >> 5;
  f32x16 acc[MB][NB];
#pragma unroll
  for (int n = 0; n < NB; ++n) acc[0][n] = f32x16{};
  gemm_loop<BM, BN, MB, NB, false>(Xg, Wg, DM, smem, acc, wm, wn, tid);
  const int tok = tm * BM + wm * 32 + r;
#pragma unroll
  for (int n = 0; n < NB; ++n) {
    u16* dst = out + (size_t)tok * 1024 + br * 512 + tn * BN + wn * 64 + n * 32 + 4 * h;
#pragma unroll
    for (int g4 = 0; g4 < 4; ++g4) {
      u32x2 v = {cvtpk(acc[0][n][4 * g4], acc[0][n][4 * g4 + 1]), cvtpk(acc[0][n][4 * g4 + 2], acc[0][n][4 * g4 + 3])};
      *(u32x2*)(dst + 8 * g4) = v;
    }
  }
}

struct AttnState { f32x16 o; f32x16 negm; float m; };

template <int MASK>
DI void attn_tile(const char* kp, const char* vp0, const char* vp1, bf16x8 qf, AttnState& st, int r, int h, bool first) {
  bf16x8 kf = *(const bf16x8*)kp;
  bf16x8 v0 = *(const bf16x8*)vp0;
  bf16x8 v1 = *(const bf16x8*)vp1;
  f32x16 s = MFMA(kf, qf, st.negm);
  if (MASK) {
#pragma unroll
    for (int i = 0; i < 16; ++i) {
      int j = (i & 3) + 8 * (i >> 2) + 4 * h;
      bool keep = MASK == 1 ? (j > r) : (j <= r);
      s[i] = keep ? s[i] : -INFINITY;
    }
  }
  float mx = __builtin_fmaxf(__builtin_fmaxf(s[0], s[1]), s[2]);
#pragma unroll
  for (int i = 3; i < 15; i += 2) mx = __builtin_fmaxf(__builtin_fmaxf(mx, s[i]), s[i + 1]);
  mx = __builtin_fmaxf(mx, s[15]);
  if (first || __any(mx > THR)) {
    auto rr = __builtin_amdgcn_permlane32_swap(__float_as_uint(mx), __float_as_uint(mx), false, false);
    float rm = __builtin_fmaxf(__uint_as_float(rr[0]), __uint_as_float(rr[1]));
    float dl = first ? rm : __builtin_fmaxf(rm, 0.f);
    float f = first ? 0.f : __builtin_amdgcn_exp2f(-dl);
    st.m += dl;
#pragma unroll
    for (int i = 0; i < 16; ++i) { s[i] -= dl; st.o[i] *= f; st.negm[i] = -st.m; }
  }
#pragma unroll
  for (int i = 0; i < 16; ++i) s[i] = __builtin_amdgcn_exp2f(s[i]);
  u32x4 p0 = {cvtpk(s[0], s[1]), cvtpk(s[2], s[3]), cvtpk(s[4], s[5]), cvtpk(s[6], s[7])};
  u32x4 p1 = {cvtpk(s[8], s[9]), cvtpk(s[10], s[11]), cvtpk(s[12], s[13]), cvtpk(s[14], s[15])};
  st.o = MFMA(v0, __builtin_bit_cast(bf16x8, p0), st.o);
  st.o = MFMA(v1, __builtin_bit_cast(bf16x8, p1), st.o);
}

DI void attn_tile4(const char* kp, const char* vp0, const char* vp1, int vs, bf16x8 qf, AttnState& st, bool first) {
  f32x16 s[4];
#pragma unroll
  for (int u = 0; u < 4; ++u) s[u] = MFMA(*(const bf16x8*)(kp + u * 1024), qf, st.negm);
  float mx = -INFINITY;
#pragma unroll
  for (int u = 0; u < 4; ++u)
#pragma unroll
    for (int i = 0; i < 16; i += 2) mx = __builtin_fmaxf(__builtin_fmaxf(mx, s[u][i]), s[u][i + 1]);
  if (first || __any(mx > THR)) {
    auto rr = __builtin_amdgcn_permlane32_swap(__float_as_uint(mx), __float_as_uint(mx), false, false);
    float rm = __builtin_fmaxf(__uint_as_float(rr[0]), __uint_as_float(rr[1]));
    float dl = first ? rm : __builtin_fmaxf(rm, 0.f);
    float f = first ? 0.f : __builtin_amdgcn_exp2f(-dl);
    st.m += dl;
#pragma unroll
    for (int i = 0; i < 16; ++i) { s[0][i] -= dl; s[1][i] -= dl; s[2][i] -= dl; s[3][i] -= dl; st.o[i] *= f; st.negm[i] = -st.m; }
  }
#pragma unroll
  for (int u = 0; u < 4; ++u) {
#pragma unroll
    for (int i = 0; i < 16; ++i) s[u][i] = __builtin_amdgcn_exp2f(s[u][i]);
    u32x4 p0 = {cvtpk(s[u][0], s[u][1]), cvtpk(s[u][2], s[u][3]), cvtpk(s[u][4], s[u][5]), cvtpk(s[u][6], s[u][7])};
    u32x4 p1 = {cvtpk(s[u][8], s[u][9]), cvtpk(s[u][10], s[u][11]), cvtpk(s[u][12], s[u][13]), cvtpk(s[u][14], s[u][15])};
    st.o = MFMA(*(const bf16x8*)(vp0 + u * vs), __builtin_bit_cast(bf16x8, p0), st.o);
    st.o = MFMA(*(const bf16x8*)(vp1 + u * vs), __builtin_bit_cast(bf16x8, p1), st.o);
  }
}

DI void attn_store(const AttnState& st, u16* dst) {
  float inv = 1.0f / st.o[8];
  u32x2 a = {cvtpk(st.o[0] * inv, st.o[1] * inv), cvtpk(st.o[2] * inv, st.o[3] * inv)};
  u32x2 b = {cvtpk(st.o[4] * inv, st.o[5] * inv), cvtpk(st.o[6] * inv, st.o[7] * inv)};
  *(u32x2*)dst = a;
  *(u32x2*)(dst + 8) = b;
}

__global__ __launch_bounds__(512) void attn_kernel(const u16* __restrict__ Qi, const u16* __restrict__ Ki, const u16* __restrict__ Vi,
                                                   u16* __restrict__ O) {
  extern __shared__ __attribute__((aligned(16))) char smem[];
  char* sK = smem;
  char* sV = smem + 65536;
  char* sC = smem + 131072;
  const int tid = threadIdx.x, lane = tid & 63, wid = __builtin_amdgcn_readfirstlane(tid >> 6);
  const int head = (blockIdx.x & 7) * 4 + (blockIdx.x >> 6), qblk = (blockIdx.x >> 3) & 7, r = lane & 31, h = lane >> 5;
  const int q0 = qblk * 256 + wid * 32;
  if (tid < 16) ((u16*)sC)[tid] = tid < 8 ? (u16)0x3F80 : (u16)0;
  const int koff = r * 32 + h * 16;
  const bool vrow = (r < 16);
  const char* vbase0 = vrow ? sV + r * 32 + h * 16 : sC + ((r == 16 || r == 20) ? 0 : 16);
  const char* vbase1 = vrow ? vbase0 + 512 : vbase0;
  const int vstride = vrow ? 1024 : 0;
  {
    const u16* Kg = Ki + (size_t)(NH + head) * SEQ * 16;
    const u16* Vg = Vi + (size_t)(NH + head) * SEQ * 16;
    bf16x8 qf = *(const bf16x8*)(Qi + ((size_t)(NH + head) * SEQ + q0 + r) * 16 + h * 8);
#pragma unroll
    for (int c = 0; c < 8; ++c) {
      int off = c * 8192 + wid * 1024;
      glds16((const char*)Kg + off + lane * 16, sK + off);
      glds16((const char*)Vg + off + lane * 16, sV + off);
    }
    AttnState st; st.o = f32x16{}; st.negm = f32x16{}; st.m = 0.f;
#pragma unroll
    for (int c = 0; c < 8; ++c) {
      if (c == 0) asm volatile("s_waitcnt vmcnt(14) lgkmcnt(0)" ::: "memory");
      else if (c == 1) asm volatile("s_waitcnt vmcnt(12)" ::: "memory");
      else if (c == 2) asm volatile("s_waitcnt vmcnt(10)" ::: "memory");
      else if (c == 3) asm volatile("s_waitcnt vmcnt(8)" ::: "memory");
      else if (c == 4) asm volatile("s_waitcnt vmcnt(6)" ::: "memory");
      else if (c == 5) asm volatile("s_waitcnt vmcnt(4)" ::: "memory");
      else if (c == 6) asm volatile("s_waitcnt vmcnt(2)" ::: "memory");
      else asm volatile("s_waitcnt vmcnt(0)" ::: "memory");
      __builtin_amdgcn_s_barrier();
#pragma unroll
      for (int tt = 0; tt < 8; tt += 4) {
        int tile = c * 8 + tt;
        attn_tile4(sK + tile * 1024 + koff, vbase0 + tile * vstride, vbase1 + tile * vstride, vstride, qf, st, tile == 0);
      }
    }
    attn_store(st, O + ((size_t)SEQ + q0 + r) * DM + head * 16 + 4 * h);
  }
  __syncthreads();
  {
    const int kt0 = qblk * 8 - 8;
    const u16* Kg = Ki + (size_t)head * SEQ * 16;
    const u16* Vg = Vi + (size_t)head * SEQ * 16;
    bf16x8 qf = *(const bf16x8*)(Qi + ((size_t)head * SEQ + q0 + r) * 16 + h * 8);
#pragma unroll
    for (int i = 0; i < 2; ++i) {
      int lt = i * 8 + wid, gt = kt0 + lt;
      if (gt >= 0) {
        glds16((const char*)Kg + (size_t)gt * 1024 + lane * 16, sK + lt * 1024);
        glds16((const char*)Vg + (size_t)gt * 1024 + lane * 16, sV + lt * 1024);
      }
    }
    asm volatile("s_waitcnt vmcnt(0)" ::: "memory");
    __syncthreads();
    AttnState st; st.o = f32x16{}; st.negm = f32x16{}; st.m = 0.f;
    const int lt_last = wid + 8;
    attn_tile<2>(sK + lt_last * 1024 + koff, vbase0 + lt_last * vstride, vbase1 + lt_last * vstride, qf, st, r, h, true);
    if (kt0 + wid >= 0) attn_tile<1>(sK + wid * 1024 + koff, vbase0 + wid * vstride, vbase1 + wid * vstride, qf, st, r, h, false);
    for (int lt = wid + 1; lt < lt_last; ++lt) {
      if (kt0 + lt >= 0) attn_tile<0>(sK + lt * 1024 + koff, vbase0 + lt * vstride, vbase1 + lt * vstride, qf, st, r, h, false);
    }
    attn_store(st, O + ((size_t)q0 + r) * DM + head * 16 + 4 * h);
  }
}

extern "C" void kernel_launch(void* const* d_in, const int* in_sizes, int n_in, void* d_out, int out_size, void* d_ws, size_t ws_size,
                              hipStream_t stream) {
  const u16* xA = (const u16*)d_in[0]; const u16* wA = (const u16*)d_in[1]; const u16* woA = (const u16*)d_in[2];
  const u16* xB = (const u16*)d_in[3]; const u16* wB = (const u16*)d_in[4]; const u16* woB = (const u16*)d_in[5];
  const size_t T = (size_t)2 * SEQ * DM;
  u16* Q = (u16*)d_ws; u16* K = Q + T; u16* V = K + T; u16* O = V + T;
  hipFuncSetAttribute((const void*)qkv_kernel, hipFuncAttributeMaxDynamicSharedMemorySize, 4 * (128 * 128 + 64 * 192 * 2));
  hipFuncSetAttribute((const void*)attn_kernel, hipFuncAttributeMaxDynamicSharedMemorySize, 131072 + 64);
  hipFuncSetAttribute((const void*)oproj_kernel, hipFuncAttributeMaxDynamicSharedMemorySize, 4 * (64 * 128 + 64 * 128 * 2));
  qkv_kernel<<<256, 384, 4 * (128 * 128 + 64 * 192 * 2), stream>>>(xA, wA, xB, wB, Q, K, V);
  attn_kernel<<<256, 512, 131072 + 64, stream>>>(Q, K, V, O);
  oproj_kernel<<<256, 256, 4 * (64 * 128 + 64 * 128 * 2), stream>>>(O, woA, woB, (u16*)d_out);
}
```
